```python
import torch

def _softmax(x, axis=-1):
    return torch.softmax(x, dim=axis)
def _gelu(x):
    return torch.nn.functional.gelu(x, approximate="tanh")
def _silu(x):
    return torch.nn.functional.silu(x)
def _sigmoid(x):
    return torch.sigmoid(x)
def _layernorm(x, axis=-1, eps=1e-5):
    m = x.mean(dim=axis, keepdim=True)
    v = x.var(dim=axis, keepdim=True, unbiased=False)
    return (x - m) / torch.sqrt(v + eps)
def _scatter_add(tgt, idx, src, axis=0):
    return tgt.clone().index_add_(axis, idx, src)
def _segment_sum(src, seg, n):
    out = torch.zeros((n,) + src.shape[1:], dtype=src.dtype)
    return out.index_add_(0, seg, src)
def _pad(x, pw):
    flat = tuple(v for pair in reversed(pw) for v in pair)
    return torch.nn.functional.pad(x, flat)

kernel_name = 'sequential_preLN__linear_kernel_hedgehog__dense_activation_silu__prompt'

def setup_inputs():

    _w = (lambda a: (lambda t: t.to(torch.bfloat16) if t.is_floating_point() else t)(torch.as_tensor(a)))
    def _A_setup():
        B, S, D, H = 1, 2048, 640, 32
        Dh = D // H
        return {
            "x": _w((lambda *s: torch.randn(*s, dtype=torch.bfloat16) * 0.05)(B, S, D)),
            "wqkv": _w((lambda *s: torch.randn(*s, dtype=torch.bfloat16) * 0.2)(D, 3 * D)),
            "w_phi": _w((lambda *s: torch.randn(*s, dtype=torch.bfloat16) * 0.2)(H, Dh, Dh) * (Dh ** -0.5)),
            "wo": _w((lambda *s: torch.randn(*s, dtype=torch.bfloat16) * 0.05)(D, D)),
        }
    _A_in = _A_setup()

    def _B_setup():
        B, S, D, F = 1, 2048, 640, 1728
        return {
            "x": _w((lambda *s: torch.randn(*s, dtype=torch.bfloat16) * 0.05)(B, S, D)),
            "w1": _w((lambda *s: torch.randn(*s, dtype=torch.bfloat16) * 1.6)(D, F) * (D ** -0.5)),
            "w2": _w((lambda *s: torch.randn(*s, dtype=torch.bfloat16) * 0.2)(F, D) * (F ** -0.5)),
        }
    _B_in = _B_setup()

    return {
        "A_x": _A_in["x"],
        "A_wqkv": _A_in["wqkv"],
        "A_w_phi": _A_in["w_phi"],
        "A_wo": _A_in["wo"],
        "B_x": _B_in["x"],
        "B_w1": _B_in["w1"],
        "B_w2": _B_in["w2"],
    }

def reference(A_x, A_wqkv, A_w_phi, A_wo, B_x, B_w1, B_w2):
    def _A_ref(x, wqkv, w_phi, wo):
        B, S, D = x.shape
        H = 32; Dh = D // H
        qkv = (lambda x, *d: x.permute(*d))((x @ wqkv).reshape(B, S, 3, H, Dh), 2, 0, 3, 1, 4)
        q, k, v = qkv[0], qkv[1], qkv[2]
        pq = torch.einsum("bhsd,hdf->bhsf", q, w_phi)
        pk = torch.einsum("bhsd,hdf->bhsf", k, w_phi)
        phi_q = torch.concatenate([_softmax(pq, axis=-1), _softmax(-pq, axis=-1)], axis=-1)
        phi_k = torch.concatenate([_softmax(pk, axis=-1), _softmax(-pk, axis=-1)], axis=-1)
        kv = torch.einsum("bhsf,bhsd->bhfd", phi_k, v)
        z  = torch.einsum("bhsf->bhf", phi_k)
        num = torch.einsum("bhqf,bhfd->bhqd", phi_q, kv)
        den = torch.einsum("bhqf,bhf->bhq", phi_q, z)[..., None] + 1e-6
        o = num / den
        return (lambda x, *d: x.permute(*d))(o, 0, 2, 1, 3).reshape(B, S, D) @ wo

    def _B_ref(x, w1, w2):
        h = torch.einsum("bsd,df->bsf", x, w1)
        h = _silu(h)
        out = torch.einsum("bsf,fd->bsd", h, w2)
        return out

    x = A_x
    h = x + _A_ref(_layernorm(x, axis=-1), A_wqkv, A_w_phi, A_wo)
    h2 = h + _B_ref(_layernorm(h, axis=-1), B_w1, B_w2)
    return h2
```

```cpp
#include <hip/hip_runtime.h>
#include <hip/hip_bf16.h>
#include <stdint.h>

typedef __attribute__((ext_vector_type(8))) short bf16x8;
typedef __attribute__((ext_vector_type(4))) short bf16x4;
typedef __attribute__((ext_vector_type(4))) float f32x4;
typedef __attribute__((ext_vector_type(4))) unsigned u32x4;
typedef __attribute__((ext_vector_type(2))) unsigned u32x2;
typedef unsigned short u16;

#define LDS_AS __attribute__((address_space(3)))

constexpr int S_ = 2048, D_ = 640, H_ = 32, DH_ = 20, FF_ = 1728;

__device__ __forceinline__ u16 f2bf(float f) {
  __hip_bfloat16 b = __float2bfloat16(f);
  return *reinterpret_cast<u16*>(&b);
}
__device__ __forceinline__ float bf2f(u16 v) { return __uint_as_float(((unsigned)v) << 16); }
__device__ __forceinline__ float rbf(float f) { return bf2f(f2bf(f)); }
__device__ __forceinline__ unsigned pack2(float a, float b) {
  return (unsigned)f2bf(a) | ((unsigned)f2bf(b) << 16);
}

__device__ __forceinline__ bf16x4 tr_read(const void* lds_ptr) {
  return __builtin_amdgcn_ds_read_tr16_b64_v4i16((LDS_AS bf16x4*)lds_ptr);
}
__device__ __forceinline__ void glds16(const void* g, void* lds_wave_base) {
  __builtin_amdgcn_global_load_lds((const __attribute__((address_space(1))) unsigned*)g,
                                   (LDS_AS unsigned*)lds_wave_base, 16, 0, 0);
}
template <int N>
__device__ __forceinline__ void wait_vm() {
  asm volatile("s_waitcnt vmcnt(%0)" ::"n"(N) : "memory");
}
__device__ __forceinline__ void raw_barrier() {
  asm volatile("" ::: "memory");
  __builtin_amdgcn_s_barrier();
  asm volatile("" ::: "memory");
}
__device__ __forceinline__ f32x4 mfma16(bf16x8 a, bf16x8 b, f32x4 c) {
  return __builtin_amdgcn_mfma_f32_16x16x32_bf16(a, b, c, 0, 0, 0);
}

__device__ __forceinline__ int a640_off(int row, int col) {
  int c = col >> 3;
  int cs = (c & ~15) | ((c ^ row) & 15);
  return row * 1280 + cs * 16 + (col & 7) * 2;
}

__device__ __forceinline__ void tile_map(int bid, int& rt, int& ct) {
  int xcd = bid & 7, idx = bid >> 3;
  rt = (xcd >> 1) * 8 + (idx & 7);
  ct = (xcd & 1) * 4 + (idx >> 3);
}

template <int NW>
__device__ __forceinline__ void ln_stage(const u16* __restrict__ src, char* As, int wave, int lane) {
  const int l16 = lane & 15;
  for (int r = wave * 4 + (lane >> 4); r < 64; r += NW * 4) {
    const u16* p = src + (size_t)r * 640;
    float v[5][8];
    float s = 0.f;
#pragma unroll
    for (int j = 0; j < 5; ++j) {
      u32x4 w = *reinterpret_cast<const u32x4*>(p + (j * 16 + l16) * 8);
#pragma unroll
      for (int e = 0; e < 4; ++e) {
        v[j][2 * e] = __uint_as_float(w[e] << 16);
        v[j][2 * e + 1] = __uint_as_float(w[e] & 0xffff0000u);
        s += v[j][2 * e] + v[j][2 * e + 1];
      }
    }
    s += __shfl_xor(s, 1); s += __shfl_xor(s, 2); s += __shfl_xor(s, 4); s += __shfl_xor(s, 8);
    const float mean = s * (1.0f / 640.0f);
    float q = 0.f;
#pragma unroll
    for (int j = 0; j < 5; ++j)
#pragma unroll
      for (int e = 0; e < 8; ++e) { float d = v[j][e] - mean; q += d * d; }
    q += __shfl_xor(q, 1); q += __shfl_xor(q, 2); q += __shfl_xor(q, 4); q += __shfl_xor(q, 8);
    const float rstd = 1.0f / sqrtf(q * (1.0f / 640.0f) + 1e-5f);
#pragma unroll
    for (int j = 0; j < 5; ++j) {
      u32x4 w;
#pragma unroll
      for (int e = 0; e < 4; ++e)
        w[e] = pack2((v[j][2 * e] - mean) * rstd, (v[j][2 * e + 1] - mean) * rstd);
      *reinterpret_cast<u32x4*>(As + a640_off(r, (j * 16 + l16) * 8)) = w;
    }
  }
}

constexpr int K1_B_OFF = 81920;
constexpr int K1_BSTG = 32 * 480;
constexpr int K1_NBUF = 4;
constexpr int K1_WP_OFF = K1_B_OFF + K1_NBUF * K1_BSTG;
constexpr int K1_LDS = K1_WP_OFF + 8192;

__global__ __launch_bounds__(320) void k1_qkv(const u16* __restrict__ x, const u16* __restrict__ wqkv,
                                              const u16* __restrict__ wphi, u16* __restrict__ phiq,
                                              float* __restrict__ kvpart) {
  extern __shared__ __attribute__((aligned(16))) char smem[];
  const int tid = threadIdx.x, wave = tid >> 6, lane = tid & 63;
  const int g = lane >> 4, l16 = lane & 15;
  int rt, ct;
  tile_map(blockIdx.x, rt, ct);
  char* As = smem;
  char* Bs = smem + K1_B_OFF;
  u16* WpT = reinterpret_cast<u16*>(smem + K1_WP_OFF);

  auto issue = [&](int c) {
    char* buf = Bs + (c & (K1_NBUF - 1)) * K1_BSTG;
#pragma unroll
    for (int i = 0; i < 3; ++i) {
      int ins = wave * 3 + i;
      int p = ins * 64 + lane;
      int kp = p / 30, cc = p % 30;
      int k = (kp & ~12) | ((kp & 4) << 1) | ((kp & 8) >> 1);
      int part = cc / 10, cw = cc % 10;
      const u16* gp = wqkv + (size_t)(c * 32 + k) * 1920 + part * 640 + ct * 80 + cw * 8;
      glds16(gp, buf + ins * 1024);
    }
  };
  issue(0); issue(1); issue(2);

  for (int i = tid; i < 4 * 32 * 32; i += 320) {
    int hh = i >> 10, f = (i >> 5) & 31, d = i & 31;
    u16 v = 0;
    if (f < 20 && d < 20) v = wphi[((ct * 4 + hh) * 20 + d) * 20 + f];
    WpT[i] = v;
  }
  ln_stage<5>(x + (size_t)rt * 64 * 640, As, wave, lane);

  f32x4 acc[4][3];
#pragma unroll
  for (int m = 0; m < 4; ++m)
#pragma unroll
    for (int n = 0; n < 3; ++n) acc[m][n] = f32x4{0.f, 0.f, 0.f, 0.f};

  const int q4 = (lane & 15) >> 2, p4 = lane & 3;
  const int krow = 16 * (g >> 1) + 4 * (g & 1) + q4;
  __syncthreads();

  for (int c = 0; c < 20; ++c) {
    if (c + K1_NBUF - 2 < 20) wait_vm<3 * (K1_NBUF - 2)>(); else wait_vm<0>();
    raw_barrier();
    if (c + K1_NBUF - 1 < 20) issue(c + K1_NBUF - 1);
    const char* buf = Bs + (c & (K1_NBUF - 1)) * K1_BSTG;
    bf16x8 a[4];
#pragma unroll
    for (int m = 0; m < 4; ++m)
      a[m] = *reinterpret_cast<const bf16x8*>(As + a640_off(m * 16 + l16, c * 32 + g * 8));
#pragma unroll
    for (int n = 0; n < 3; ++n) {
      const char* bp = buf + krow * 480 + ((wave * 3 + n) * 16 + 4 * p4) * 2;
      bf16x4 b0 = tr_read(bp), b1 = tr_read(bp + 8 * 480);
      bf16x8 b = {b0[0], b0[1], b0[2], b0[3], b1[0], b1[1], b1[2], b1[3]};
#pragma unroll
      for (int m = 0; m < 4; ++m) acc[m][n] = mfma16(a[m], b, acc[m][n]);
    }
  }
  __syncthreads();

  u16* Qs = reinterpret_cast<u16*>(smem);
  u16* Ks = reinterpret_cast<u16*>(smem + 16384);
  u16* Vt = reinterpret_cast<u16*>(smem + 32768);
  u16* PkT = reinterpret_cast<u16*>(smem + 49152);
  for (int i = tid; i < 2048; i += 320) reinterpret_cast<u32x4*>(smem)[i] = u32x4{0, 0, 0, 0};
  for (int i = tid; i < 256; i += 320) Vt[((i >> 6) * 32 + 20) * 64 + (i & 63)] = 0x3f80;
  __syncthreads();
#pragma unroll
  for (int n = 0; n < 3; ++n) {
    int nt = wave * 3 + n;
    int part = nt / 5;
    int col = (nt % 5) * 16 + l16;
    int hh = col / 20, d = col % 20;
#pragma unroll
    for (int m = 0; m < 4; ++m) {
      if (part < 2) {
        u16* X = part == 0 ? Qs : Ks;
#pragma unroll
        for (int r = 0; r < 4; ++r) {
          int s = m * 16 + 4 * g + r;
          int ch = (hh * 4 + (d >> 3)) ^ (s & 15);
          X[s * 128 + ch * 8 + (d & 7)] = f2bf(acc[m][n][r]);
        }
      } else {
        u32x2 w = {pack2(acc[m][n][0], acc[m][n][1]), pack2(acc[m][n][2], acc[m][n][3])};
        *reinterpret_cast<u32x2*>(&Vt[(hh * 32 + d) * 64 + m * 16 + 4 * g]) = w;
      }
    }
  }
  __syncthreads();
  for (int t = wave; t < 32; t += 5) {
    int which = t >> 4, hh = (t >> 2) & 3, st = t & 3;
    const u16* X = which == 0 ? Qs : Ks;
    int s = st * 16 + l16;
    bf16x8 b = *reinterpret_cast<const bf16x8*>(&X[s * 128 + ((hh * 4 + g) ^ (s & 15)) * 8]);
    bf16x8 a0 = *reinterpret_cast<const bf16x8*>(&WpT[(hh * 32 + l16) * 32 + g * 8]);
    bf16x8 a1 = *reinterpret_cast<const bf16x8*>(&WpT[(hh * 32 + 16 + l16) * 32 + g * 8]);
    f32x4 c0 = mfma16(a0, b, f32x4{0.f, 0.f, 0.f, 0.f});
    f32x4 c1 = mfma16(a1, b, f32x4{0.f, 0.f, 0.f, 0.f});
    float p0[4], p1[4];
    float mx = -3.0e38f, mn = 3.0e38f;
#pragma unroll
    for (int r = 0; r < 4; ++r) {
      p0[r] = rbf(c0[r]); p1[r] = rbf(c1[r]);
      mx = fmaxf(mx, p0[r]); mn = fminf(mn, p0[r]);
      if (g == 0) { mx = fmaxf(mx, p1[r]); mn = fminf(mn, p1[r]); }
    }
    mx = fmaxf(mx, __shfl_xor(mx, 16)); mx = fmaxf(mx, __shfl_xor(mx, 32));
    mn = fminf(mn, __shfl_xor(mn, 16)); mn = fminf(mn, __shfl_xor(mn, 32));
    float ep0[4], en0[4], ep1[4], en1[4];
    float sp = 0.f, sn = 0.f;
#pragma unroll
    for (int r = 0; r < 4; ++r) {
      ep0[r] = __expf(p0[r] - mx); en0[r] = __expf(mn - p0[r]);
      ep1[r] = g == 0 ? __expf(p1[r] - mx) : 0.f; en1[r] = g == 0 ? __expf(mn - p1[r]) : 0.f;
      sp += ep0[r] + ep1[r]; sn += en0[r] + en1[r];
    }
    sp += __shfl_xor(sp, 16); sp += __shfl_xor(sp, 32);
    sn += __shfl_xor(sn, 16); sn += __shfl_xor(sn, 32);
    const float ip = 1.0f / sp, in_ = 1.0f / sn;
    if (which == 0) {
      u16* dst = phiq + ((size_t)(rt * 64 + s) * 32 + ct * 4 + hh) * 40;
      u32x2 w;
      w = u32x2{pack2(ep0[0] * ip, ep0[1] * ip), pack2(ep0[2] * ip, ep0[3] * ip)};
      *reinterpret_cast<u32x2*>(dst + 4 * g) = w;
      w = u32x2{pack2(en0[0] * in_, en0[1] * in_), pack2(en0[2] * in_, en0[3] * in_)};
      *reinterpret_cast<u32x2*>(dst + 20 + 4 * g) = w;
      if (g == 0) {
        w = u32x2{pack2(ep1[0] * ip, ep1[1] * ip), pack2(ep1[2] * ip, ep1[3] * ip)};
        *reinterpret_cast<u32x2*>(dst + 16) = w;
        w = u32x2{pack2(en1[0] * in_, en1[1] * in_), pack2(en1[2] * in_, en1[3] * in_)};
        *reinterpret_cast<u32x2*>(dst + 36) = w;
      }
    } else {
#pragma unroll
      for (int r = 0; r < 4; ++r) {
        PkT[(hh * 48 + 4 * g + r) * 64 + s] = f2bf(ep0[r] * ip);
        PkT[(hh * 48 + 20 + 4 * g + r) * 64 + s] = f2bf(en0[r] * in_);
        if (g == 0) {
          PkT[(hh * 48 + 16 + r) * 64 + s] = f2bf(ep1[r] * ip);
          PkT[(hh * 48 + 36 + r) * 64 + s] = f2bf(en1[r] * in_);
        }
      }
    }
  }
  __syncthreads();
  for (int t = wave; t < 24; t += 5) {
    int hh = t / 6, mt = (t % 6) >> 1, nt = t & 1;
    f32x4 c = {0.f, 0.f, 0.f, 0.f};
#pragma unroll
    for (int ks = 0; ks < 2; ++ks) {
      bf16x8 a = *reinterpret_cast<const bf16x8*>(&PkT[(hh * 48 + mt * 16 + l16) * 64 + ks * 32 + g * 8]);
      bf16x8 b = *reinterpret_cast<const bf16x8*>(&Vt[(hh * 32 + nt * 16 + l16) * 64 + ks * 32 + g * 8]);
      c = mfma16(a, b, c);
    }
    int d = nt * 16 + l16;
    if (d < 21) {
#pragma unroll
      for (int r = 0; r < 4; ++r) {
        int f = mt * 16 + 4 * g + r;
        if (f < 40) kvpart[(((size_t)rt * 32 + ct * 4 + hh) * 21 + d) * 40 + f] = c[r];
      }
    }
  }
}

__global__ __launch_bounds__(256) void kr_reduce(const float* __restrict__ kvpart, u16* __restrict__ kvzT) {
  int idx = blockIdx.x * 256 + threadIdx.x;
  int h = idx >> 11, d = (idx >> 6) & 31, f = idx & 63;
  float s = 0.f;
  if (d < 21 && f < 40) {
    const float* p = kvpart + ((size_t)h * 21 + d) * 40 + f;
#pragma unroll 8
    for (int rt = 0; rt < 32; ++rt) s += p[(size_t)rt * 32 * 840];
  }
  kvzT[idx] = f2bf(s);
}

constexpr int K2_B_OFF = 81920;
constexpr int K2_BSTG = 64 * 160;
constexpr int K2_NBUF = 5;
constexpr int K2_LDS = K2_B_OFF + K2_NBUF * K2_BSTG;

__global__ __launch_bounds__(256) void k2_attn_out(const u16* __restrict__ phiq, const u16* __restrict__ kvzT,
                                                   const u16* __restrict__ wo, const u16* __restrict__ x,
                                                   u16* __restrict__ hbuf) {
  extern __shared__ __attribute__((aligned(16))) char smem[];
  const int tid = threadIdx.x, wave = tid >> 6, lane = tid & 63;
  const int g = lane >> 4, l16 = lane & 15;
  int rt, ct;
  tile_map(blockIdx.x, rt, ct);
  char* As = smem;
  char* Bs = smem + K2_B_OFF;

  auto issue = [&](int c) {
    char* buf = Bs + (c % K2_NBUF) * K2_BSTG;
#pragma unroll
    for (int i = 0; i < 3; ++i) {
      int ins = i * 4 + wave;
      if (ins < 10) {
        int p = ins * 64 + lane;
        int kp = p / 10, cw = p % 10;
        int k = (kp & ~12) | ((kp & 4) << 1) | ((kp & 8) >> 1);
        const u16* gp = wo + (size_t)(c * 64 + k) * 640 + ct * 80 + cw * 8;
        glds16(gp, buf + ins * 1024);
      }
    }
  };
  issue(0); issue(1); issue(2); issue(3);

  for (int h = wave; h < 32; h += 4) {
    bf16x8 ka[2][2];
#pragma unroll
    for (int mt = 0; mt < 2; ++mt)
#pragma unroll
      for (int ks = 0; ks < 2; ++ks)
        ka[mt][ks] = *reinterpret_cast<const bf16x8*>(kvzT + ((size_t)h * 32 + mt * 16 + l16) * 64 + ks * 32 + g * 8);
#pragma unroll
    for (int st = 0; st < 4; ++st) {
      int s = st * 16 + l16;
      const u16* pq = phiq + ((size_t)(rt * 64 + s) * 32 + h) * 40;
      bf16x8 b0 = *reinterpret_cast<const bf16x8*>(pq + g * 8);
      bf16x8 b1 = {0, 0, 0, 0, 0, 0, 0, 0};
      if (g == 0) b1 = *reinterpret_cast<const bf16x8*>(pq + 32);
      f32x4 c0 = {0.f, 0.f, 0.f, 0.f}, c1 = {0.f, 0.f, 0.f, 0.f};
      c0 = mfma16(ka[0][0], b0, c0); c0 = mfma16(ka[0][1], b1, c0);
      c1 = mfma16(ka[1][0], b0, c1); c1 = mfma16(ka[1][1], b1, c1);
      float den = rbf(rbf(__shfl(c1[0], 16 + l16)) + 1e-6f);
      float inv = 1.0f / den;
      u32x2 w = {pack2(rbf(c0[0]) * inv, rbf(c0[1]) * inv), pack2(rbf(c0[2]) * inv, rbf(c0[3]) * inv)};
      *reinterpret_cast<u32x2*>(As + a640_off(s, h * 20 + 4 * g)) = w;
      if (g == 0) {
        u32x2 w2 = {pack2(rbf(c1[0]) * inv, rbf(c1[1]) * inv), pack2(rbf(c1[2]) * inv, rbf(c1[3]) * inv)};
        *reinterpret_cast<u32x2*>(As + a640_off(s, h * 20 + 16)) = w2;
      }
    }
  }

  f32x4 acc[5];
#pragma unroll
  for (int n = 0; n < 5; ++n) acc[n] = f32x4{0.f, 0.f, 0.f, 0.f};
  const int q4 = (lane & 15) >> 2, p4 = lane & 3;
  const int krow = 16 * (g >> 1) + 4 * (g & 1) + q4;
  __syncthreads();

  for (int c = 0; c < 10; ++c) {
    if (c + K2_NBUF - 2 < 10) {
      if (wave < 2) wait_vm<3 * (K2_NBUF - 2)>(); else wait_vm<2 * (K2_NBUF - 2)>();
    } else wait_vm<0>();
    raw_barrier();
    if (c + K2_NBUF - 1 < 10) issue(c + K2_NBUF - 1);
    const char* buf = Bs + (c % K2_NBUF) * K2_BSTG;
#pragma unroll
    for (int ks = 0; ks < 2; ++ks) {
      bf16x8 a = *reinterpret_cast<const bf16x8*>(As + a640_off(wave * 16 + l16, c * 64 + ks * 32 + g * 8));
#pragma unroll
      for (int n = 0; n < 5; ++n) {
        const char* bp = buf + (ks * 32 + krow) * 160 + (n * 16 + 4 * p4) * 2;
        bf16x4 b0 = tr_read(bp), b1 = tr_read(bp + 8 * 160);
        bf16x8 b = {b0[0], b0[1], b0[2], b0[3], b1[0], b1[1], b1[2], b1[3]};
        acc[n] = mfma16(a, b, acc[n]);
      }
    }
  }
#pragma unroll
  for (int n = 0; n < 5; ++n)
#pragma unroll
    for (int r = 0; r < 4; ++r) {
      size_t o = (size_t)(rt * 64 + wave * 16 + 4 * g + r) * 640 + ct * 80 + n * 16 + l16;
      hbuf[o] = f2bf(bf2f(x[o]) + rbf(acc[n][r]));
    }
}

constexpr int K3_B_OFF = 81920;
constexpr int K3_BSTG = 32 * 448;
constexpr int K3_NBUF = 5;
constexpr int K3_LDS = K3_B_OFF + K3_NBUF * K3_BSTG;

__global__ __launch_bounds__(256) void k3_ffn1(const u16* __restrict__ hbuf, const u16* __restrict__ w1,
                                               u16* __restrict__ act) {
  extern __shared__ __attribute__((aligned(16))) char smem[];
  const int tid = threadIdx.x, wave = tid >> 6, lane = tid & 63;
  const int g = lane >> 4, l16 = lane & 15;
  int rt, ct;
  tile_map(blockIdx.x, rt, ct);
  const int n0 = (ct >> 1) * 432 + (ct & 1) * 224;
  const int ntiles = (ct & 1) ? 13 : 14;
  char* As = smem;
  char* Bs = smem + K3_B_OFF;

  auto issue = [&](int c) {
    char* buf = Bs + (c % K3_NBUF) * K3_BSTG;
#pragma unroll
    for (int i = 0; i < 4; ++i) {
      int ins = i * 4 + wave;
      if (ins < 14) {
        int p = ins * 64 + lane;
        int kp = p / 28, cw = p % 28;
        int k = (kp & ~12) | ((kp & 4) << 1) | ((kp & 8) >> 1);
        int col = n0 + cw * 8;
        col = col > 1720 ? 1720 : col;
        const u16* gp = w1 + (size_t)(c * 32 + k) * 1728 + col;
        glds16(gp, buf + ins * 1024);
      }
    }
  };
  issue(0); issue(1); issue(2); issue(3);
  ln_stage<4>(hbuf + (size_t)rt * 64 * 640, As, wave, lane);

  const int nbase = wave < 2 ? wave * 4 : 8 + (wave - 2) * 3;
  f32x4 acc[4][4];
#pragma unroll
  for (int m = 0; m < 4; ++m)
#pragma unroll
    for (int n = 0; n < 4; ++n) acc[m][n] = f32x4{0.f, 0.f, 0.f, 0.f};
  const int q4 = (lane & 15) >> 2, p4 = lane & 3;
  const int krow = 16 * (g >> 1) + 4 * (g & 1) + q4;
  __syncthreads();

  for (int c = 0; c < 20; ++c) {
    if (c + K3_NBUF - 2 < 20) {
      if (wave < 2) wait_vm<4 * (K3_NBUF - 2)>(); else wait_vm<3 * (K3_NBUF - 2)>();
    } else wait_vm<0>();
    raw_barrier();
    if (c + K3_NBUF - 1 < 20) issue(c + K3_NBUF - 1);
    const char* buf = Bs + (c % K3_NBUF) * K3_BSTG;
    bf16x8 a[4];
#pragma unroll
    for (int m = 0; m < 4; ++m)
      a[m] = *reinterpret_cast<const bf16x8*>(As + a640_off(m * 16 + l16, c * 32 + g * 8));
#pragma unroll
    for (int n = 0; n < 4; ++n) {
      if (n < 3 || wave < 2) {
        const char* bp = buf + krow * 448 + ((nbase + n) * 16 + 4 * p4) * 2;
        bf16x4 b0 = tr_read(bp), b1 = tr_read(bp + 8 * 448);
        bf16x8 b = {b0[0], b0[1], b0[2], b0[3], b1[0], b1[1], b1[2], b1[3]};
#pragma unroll
        for (int m = 0; m < 4; ++m) acc[m][n] = mfma16(a[m], b, acc[m][n]);
      }
    }
  }
#pragma unroll
  for (int n = 0; n < 4; ++n) {
    int nt = nbase + n;
    if ((n < 3 || wave < 2) && nt < ntiles) {
#pragma unroll
      for (int m = 0; m < 4; ++m)
#pragma unroll
        for (int r = 0; r < 4; ++r) {
          float v = rbf(acc[m][n][r]);
          float sv = v / (1.0f + __expf(-v));
          act[(size_t)(rt * 64 + m * 16 + 4 * g + r) * 1728 + n0 + nt * 16 + l16] = f2bf(sv);
        }
    }
  }
}

constexpr int K4_STG = 8192 + 10240;
constexpr int K4_NBUF = 6;
constexpr int K4_LDS = K4_NBUF * K4_STG;

__global__ __launch_bounds__(256) void k4_ffn2(const u16* __restrict__ act, const u16* __restrict__ w2,
                                               const u16* __restrict__ hbuf, u16* __restrict__ out) {
  extern __shared__ __attribute__((aligned(16))) char smem[];
  const int tid = threadIdx.x, wave = tid >> 6, lane = tid & 63;
  const int g = lane >> 4, l16 = lane & 15;
  int rt, ct;
  tile_map(blockIdx.x, rt, ct);

  auto issue = [&](int c) {
    char* buf = smem + (c % K4_NBUF) * K4_STG;
#pragma unroll
    for (int i = 0; i < 2; ++i) {
      int ins = i * 4 + wave;
      int p = ins * 64 + lane;
      int row = p >> 3, cs = p & 7;
      const u16* gp = act + (size_t)(rt * 64 + row) * 1728 + c * 64 + ((cs ^ (row & 7)) * 8);
      glds16(gp, buf + ins * 1024);
    }
#pragma unroll
    for (int i = 0; i < 3; ++i) {
      int ins = i * 4 + wave;
      if (ins < 10) {
        int p = ins * 64 + lane;
        int kp = p / 10, cw = p % 10;
        int k = (kp & ~12) | ((kp & 4) << 1) | ((kp & 8) >> 1);
        const u16* gp = w2 + (size_t)(c * 64 + k) * 640 + ct * 80 + cw * 8;
        glds16(gp, buf + 8192 + ins * 1024);
      }
    }
  };
#pragma unroll
  for (int c = 0; c < K4_NBUF - 1; ++c) issue(c);

  f32x4 acc[5];
#pragma unroll
  for (int n = 0; n < 5; ++n) acc[n] = f32x4{0.f, 0.f, 0.f, 0.f};
  const int q4 = (lane & 15) >> 2, p4 = lane & 3;
  const int krow = 16 * (g >> 1) + 4 * (g & 1) + q4;
  const int arow = wave * 16 + l16;

  for (int c = 0; c < 27; ++c) {
    if (c + K4_NBUF - 2 < 27) {
      if (wave < 2) wait_vm<5 * (K4_NBUF - 2)>(); else wait_vm<4 * (K4_NBUF - 2)>();
    } else wait_vm<0>();
    raw_barrier();
    if (c + K4_NBUF - 1 < 27) issue(c + K4_NBUF - 1);
    const char* buf = smem + (c % K4_NBUF) * K4_STG;
#pragma unroll
    for (int ks = 0; ks < 2; ++ks) {
      bf16x8 a = *reinterpret_cast<const bf16x8*>(buf + arow * 128 + (((ks * 4 + g) ^ (arow & 7)) * 16));
#pragma unroll
      for (int n = 0; n < 5; ++n) {
        const char* bp = buf + 8192 + (ks * 32 + krow) * 160 + (n * 16 + 4 * p4) * 2;
        bf16x4 b0 = tr_read(bp), b1 = tr_read(bp + 8 * 160);
        bf16x8 b = {b0[0], b0[1], b0[2], b0[3], b1[0], b1[1], b1[2], b1[3]};
        acc[n] = mfma16(a, b, acc[n]);
      }
    }
  }
#pragma unroll
  for (int n = 0; n < 5; ++n)
#pragma unroll
    for (int r = 0; r < 4; ++r) {
      size_t o = (size_t)(rt * 64 + wave * 16 + 4 * g + r) * 640 + ct * 80 + n * 16 + l16;
      out[o] = f2bf(bf2f(hbuf[o]) + rbf(acc[n][r]));
    }
}

extern "C" void kernel_launch(void* const* d_in, const int* in_sizes, int n_in, void* d_out, int out_size,
                              void* d_ws, size_t ws_size, hipStream_t stream) {
  const u16* x = (const u16*)d_in[0];
  const u16* wqkv = (const u16*)d_in[1];
  const u16* wphi = (const u16*)d_in[2];
  const u16* wo = (const u16*)d_in[3];
  const u16* w1 = (const u16*)d_in[5];
  const u16* w2 = (const u16*)d_in[6];
  u16* out = (u16*)d_out;

  char* ws = (char*)d_ws;
  u16* phiq = (u16*)(ws);
  float* kvpart = (float*)(ws + 5242880);
  u16* kvzT = (u16*)(ws + 5242880 + 3440640);
  u16* hbuf = (u16*)(ws + 8814592);
  u16* act = (u16*)(ws + 8814592 + 2621440);

  hipFuncSetAttribute((const void*)k1_qkv, hipFuncAttributeMaxDynamicSharedMemorySize, K1_LDS);
  hipFuncSetAttribute((const void*)k2_attn_out, hipFuncAttributeMaxDynamicSharedMemorySize, K2_LDS);
  hipFuncSetAttribute((const void*)k3_ffn1, hipFuncAttributeMaxDynamicSharedMemorySize, K3_LDS);
  hipFuncSetAttribute((const void*)k4_ffn2, hipFuncAttributeMaxDynamicSharedMemorySize, K4_LDS);

  k1_qkv<<<256, 320, K1_LDS, stream>>>(x, wqkv, wphi, phiq, kvpart);
  kr_reduce<<<256, 256, 0, stream>>>(kvpart, kvzT);
  k2_attn_out<<<256, 256, K2_LDS, stream>>>(phiq, kvzT, wo, x, hbuf);
  k3_ffn1<<<256, 256, K3_LDS, stream>>>(hbuf, w1, act);
  k4_ffn2<<<256, 256, K4_LDS, stream>>>(act, w2, hbuf, out);
}
```

```cpp
#include <hip/hip_runtime.h>
#include <hip/hip_bf16.h>
#include <stdint.h>

typedef __attribute__((ext_vector_type(8))) short bf16x8;
typedef __attribute__((ext_vector_type(4))) short bf16x4;
typedef __attribute__((ext_vector_type(4))) float f32x4;
typedef __attribute__((ext_vector_type(4))) unsigned u32x4;
typedef __attribute__((ext_vector_type(2))) unsigned u32x2;
typedef unsigned short u16;

#define LDS_AS __attribute__((address_space(3)))

constexpr int S_ = 2048, D_ = 640, H_ = 32, DH_ = 20, FF_ = 1728;

__device__ __forceinline__ u16 f2bf(float f) {
  __hip_bfloat16 b = __float2bfloat16(f);
  return *reinterpret_cast<u16*>(&b);
}
__device__ __forceinline__ float bf2f(u16 v) { return __uint_as_float(((unsigned)v) << 16); }
__device__ __forceinline__ float rbf(float f) { return bf2f(f2bf(f)); }
__device__ __forceinline__ unsigned pack2(float a, float b) {
  return (unsigned)f2bf(a) | ((unsigned)f2bf(b) << 16);
}

__device__ __forceinline__ unsigned lds_addr(const void* p) {
  return (unsigned)(size_t)(LDS_AS const char*)p;
}
template <int OFF>
__device__ __forceinline__ bf16x4 tr_rd(unsigned a) {
  bf16x4 r;
  asm volatile("ds_read_b64_tr_b16 %0, %1 offset:%2" : "=v"(r) : "v"(a), "n"(OFF));
  return r;
}
template <int OFF>
__device__ __forceinline__ bf16x8 b128_rd(unsigned a) {
  bf16x8 r;
  asm volatile("ds_read_b128 %0, %1 offset:%2" : "=v"(r) : "v"(a), "n"(OFF));
  return r;
}
__device__ __forceinline__ void lds_wait() {
  asm volatile("s_waitcnt lgkmcnt(0)" ::: "memory");
  __builtin_amdgcn_sched_barrier(0);
}
__device__ __forceinline__ bf16x8 cat(bf16x4 a, bf16x4 b) {
  return bf16x8{a[0], a[1], a[2], a[3], b[0], b[1], b[2], b[3]};
}
__device__ __forceinline__ void glds16(const void* g, void* lds_wave_base) {
  __builtin_amdgcn_global_load_lds((const __attribute__((address_space(1))) unsigned*)g,
                                   (LDS_AS unsigned*)lds_wave_base, 16, 0, 0);
}
template <int N>
__device__ __forceinline__ void wait_vm() {
  asm volatile("s_waitcnt vmcnt(%0)" ::"n"(N) : "memory");
}
__device__ __forceinline__ void raw_barrier() {
  asm volatile("" ::: "memory");
  __builtin_amdgcn_s_barrier();
  asm volatile("" ::: "memory");
}
__device__ __forceinline__ f32x4 mfma16(bf16x8 a, bf16x8 b, f32x4 c) {
  return __builtin_amdgcn_mfma_f32_16x16x32_bf16(a, b, c, 0, 0, 0);
}

__device__ __forceinline__ int a640_off(int row, int col) {
  int c = col >> 3;
  int cs = (c & ~15) | ((c ^ row) & 15);
  return row * 1280 + cs * 16 + (col & 7) * 2;
}

__device__ __forceinline__ void tile_map(int bid, int& rt, int& ct) {
  int xcd = bid & 7, idx = bid >> 3;
  rt = (xcd >> 1) * 8 + (idx & 7);
  ct = (xcd & 1) * 4 + (idx >> 3);
}

template <int NW>
__device__ __forceinline__ void ln_stage(const u16* __restrict__ src, char* As, int wave, int lane) {
  const int l16 = lane & 15;
  for (int r = wave * 4 + (lane >> 4); r < 64; r += NW * 4) {
    const u16* p = src + (size_t)r * 640;
    float v[5][8];
    float s = 0.f;
#pragma unroll
    for (int j = 0; j < 5; ++j) {
      u32x4 w = *reinterpret_cast<const u32x4*>(p + (j * 16 + l16) * 8);
#pragma unroll
      for (int e = 0; e < 4; ++e) {
        v[j][2 * e] = __uint_as_float(w[e] << 16);
        v[j][2 * e + 1] = __uint_as_float(w[e] & 0xffff0000u);
        s += v[j][2 * e] + v[j][2 * e + 1];
      }
    }
    s += __shfl_xor(s, 1); s += __shfl_xor(s, 2); s += __shfl_xor(s, 4); s += __shfl_xor(s, 8);
    const float mean = s * (1.0f / 640.0f);
    float q = 0.f;
#pragma unroll
    for (int j = 0; j < 5; ++j)
#pragma unroll
      for (int e = 0; e < 8; ++e) { float d = v[j][e] - mean; q += d * d; }
    q += __shfl_xor(q, 1); q += __shfl_xor(q, 2); q += __shfl_xor(q, 4); q += __shfl_xor(q, 8);
    const float rstd = 1.0f / sqrtf(q * (1.0f / 640.0f) + 1e-5f);
#pragma unroll
    for (int j = 0; j < 5; ++j) {
      u32x4 w;
#pragma unroll
      for (int e = 0; e < 4; ++e)
        w[e] = pack2((v[j][2 * e] - mean) * rstd, (v[j][2 * e + 1] - mean) * rstd);
      *reinterpret_cast<u32x4*>(As + a640_off(r, (j * 16 + l16) * 8)) = w;
    }
  }
}

template <int RPP, int PPR, int NT, class ColFn>
__device__ __forceinline__ void l2_warm(const u16* __restrict__ W, int ldb, ColFn colfn, int part, int tid,
                                        u32x4 (&pf)[5]) {
  static_assert(RPP * PPR <= 5 * NT, "warm share too large");
#pragma unroll
  for (int i = 0; i < 5; ++i) {
    int p = tid + i * NT;
    pf[i] = u32x4{0, 0, 0, 0};
    if (p < RPP * PPR) {
      int r = p / PPR, cw = p % PPR;
      pf[i] = *reinterpret_cast<const u32x4*>(W + (size_t)(part * RPP + r) * ldb + colfn(cw));
    }
  }
}
__device__ __forceinline__ void l2_warm_done(u32x4 (&pf)[5]) {
  asm volatile("" ::"v"(pf[0]), "v"(pf[1]), "v"(pf[2]), "v"(pf[3]), "v"(pf[4]));
}
__device__ __forceinline__ int swap23(int k) { return (k & ~12) | ((k & 4) << 1) | ((k & 8) >> 1); }

template <int NC, class ColFn, class Pre>
__device__ __forceinline__ void ln_gemm_loop(char* smem, const u16* __restrict__ Arows, const u16* __restrict__ Bmat,
                                             int ldb, ColFn colfn, Pre pre, int wave, int lane, int nbase, int ncnt,
                                             f32x4 (&acc)[4][4], f32x4 (&cs)[4], float (&rsum)[2], float (&rsq)[2]) {
  constexpr int CPR = NC / 8;
  constexpr int NINS = 8 + CPR;
  constexpr int STG = 8192 + 64 * NC * 2;
  constexpr int NBUF = 4;
  const int g = lane >> 4, l16 = lane & 15;
  const u16* src[5];
  unsigned dsto[5];
#pragma unroll
  for (int i = 0; i < 5; ++i) {
    int j = wave + 8 * i;
    if (i == 0) {
      int p = j * 64 + lane;
      int row = p >> 3, c8 = p & 7;
      src[i] = Arows + (size_t)row * 640 + ((c8 ^ (row & 7)) * 8);
      dsto[i] = j * 1024;
    } else {
      int jb = (j < NINS ? j : 8) - 8;
      int p = jb * 64 + lane;
      int kp = p / CPR, cw = p % CPR;
      src[i] = Bmat + (size_t)swap23(kp) * ldb + colfn(cw);
      dsto[i] = 8192 + jb * 1024;
    }
  }
  const bool five = wave + 32 < NINS;
  auto issue = [&](int c) {
    char* buf = smem + (c & (NBUF - 1)) * STG;
    glds16(src[0] + (size_t)c * 64, buf + dsto[0]);
    glds16(src[1] + (size_t)c * 64 * ldb, buf + dsto[1]);
    glds16(src[2] + (size_t)c * 64 * ldb, buf + dsto[2]);
    glds16(src[3] + (size_t)c * 64 * ldb, buf + dsto[3]);
    if (five) glds16(src[4] + (size_t)c * 64 * ldb, buf + dsto[4]);
  };
  issue(0); issue(1); issue(2);
  pre();

  const int kh = (wave >> 2) & 1;
  const int q4 = (lane & 15) >> 2, p4 = lane & 3;
  const int krow = 32 * kh + 16 * (g >> 1) + 4 * (g & 1) + q4;
  const unsigned base = lds_addr(smem);
  const unsigned aoff = base + l16 * 128 + (((kh * 4 + g) ^ (l16 & 7)) * 16);
  const unsigned boff = base + 8192 + krow * (NC * 2) + (nbase * 16 + 4 * p4) * 2;
  const unsigned soff = base + ((lane >> 3) | (wave << 3)) * 128 + (lane & 7) * 16;
  const bf16x8 ones = {0x3f80, 0x3f80, 0x3f80, 0x3f80, 0x3f80, 0x3f80, 0x3f80, 0x3f80};

  for (int c = 0; c < 10; ++c) {
    if (c + NBUF - 2 < 10) {
      if (five) wait_vm<5 * (NBUF - 2)>(); else wait_vm<4 * (NBUF - 2)>();
    } else wait_vm<0>();
    raw_barrier();
    if (c + NBUF - 1 < 10) issue(c + NBUF - 1);
    const unsigned so = (c & (NBUF - 1)) * STG;
    {
      const unsigned aa = aoff + so, ba = boff + so;
      bf16x8 a0 = b128_rd<0>(aa), a1 = b128_rd<2048>(aa), a2 = b128_rd<4096>(aa), a3 = b128_rd<6144>(aa);
      bf16x4 t[4][2];
      t[0][0] = tr_rd<0>(ba); t[0][1] = tr_rd<8 * NC * 2>(ba);
      t[1][0] = tr_rd<32>(ba); t[1][1] = tr_rd<8 * NC * 2 + 32>(ba);
      t[2][0] = tr_rd<64>(ba); t[2][1] = tr_rd<8 * NC * 2 + 64>(ba);
      t[3][0] = tr_rd<96>(ba); t[3][1] = tr_rd<8 * NC * 2 + 96>(ba);
      bf16x8 v0 = b128_rd<0>(soff + so);
      lds_wait();
#pragma unroll
      for (int n = 0; n < 4; ++n) {
        if (n < 3 || ncnt == 4) {
          bf16x8 b = cat(t[n][0], t[n][1]);
          acc[0][n] = mfma16(a0, b, acc[0][n]);
          acc[1][n] = mfma16(a1, b, acc[1][n]);
          acc[2][n] = mfma16(a2, b, acc[2][n]);
          acc[3][n] = mfma16(a3, b, acc[3][n]);
          cs[n] = mfma16(ones, b, cs[n]);
        }
      }
#pragma unroll
      for (int e = 0; e < 8; ++e) {
        float f0 = bf2f((u16)v0[e]);
        rsum[0] += f0; rsq[0] += f0 * f0;
      }
    }
  }
}

__device__ __forceinline__ void ln_gemm_finish(char* smem, int wave, int lane, f32x4 (&acc)[4][4], f32x4 (&cs)[4],
                                               float (&rsum)[2], float (&rsq)[2]) {
  f32x4* red = reinterpret_cast<f32x4*>(smem);
  float* st = reinterpret_cast<float*>(smem + 65536);
  float* csl = reinterpret_cast<float*>(smem + 66560);
  __syncthreads();
  if (wave >= 4 && wave < 8) {
#pragma unroll
    for (int n = 0; n < 4; ++n) {
#pragma unroll
      for (int m = 0; m < 4; ++m) red[(((wave - 4) * 4 + m) * 4 + n) * 64 + lane] = acc[m][n];
      if (lane < 16) csl[((wave - 4) * 4 + n) * 16 + lane] = cs[n][0];
    }
  }
  {
    float a = rsum[0], q = rsq[0];
    a += __shfl_xor(a, 1); a += __shfl_xor(a, 2); a += __shfl_xor(a, 4);
    q += __shfl_xor(q, 1); q += __shfl_xor(q, 2); q += __shfl_xor(q, 4);
    if ((lane & 7) == 0) {
      int row = (lane >> 3) | (wave << 3);
      float mean = a * (1.0f / 640.0f);
      float var = fmaxf(q * (1.0f / 640.0f) - mean * mean, 0.f);
      st[row * 2] = mean;
      st[row * 2 + 1] = 1.0f / sqrtf(var + 1e-5f);
    }
  }
  __syncthreads();
  if (wave < 4) {
    const int g = lane >> 4, l16 = lane & 15;
#pragma unroll
    for (int n = 0; n < 4; ++n) {
      float c = cs[n][0] + csl[(wave * 4 + n) * 16 + l16];
#pragma unroll
      for (int m = 0; m < 4; ++m) {
        f32x4 v = acc[m][n] + red[((wave * 4 + m) * 4 + n) * 64 + lane];
#pragma unroll
        for (int r = 0; r < 4; ++r) {
          int row = m * 16 + 4 * g + r;
          acc[m][n][r] = st[row * 2 + 1] * (v[r] - st[row * 2] * c);
        }
      }
    }
  }
  __syncthreads();
}

constexpr int K1_LDS = 4 * (8192 + 64 * 480);

__global__ __launch_bounds__(512) void k1_qkv(const u16* __restrict__ x, const u16* __restrict__ wqkv,
                                               const u16* __restrict__ wphi, u16* __restrict__ phiq,
                                               float* __restrict__ kvpart, const u16* __restrict__ wnext) {
  extern __shared__ __attribute__((aligned(16))) char smem[];
  const int tid = threadIdx.x, wave = tid >> 6, lane = tid & 63;
  const int g = lane >> 4, l16 = lane & 15;
  int rt, ct;
  tile_map(blockIdx.x, rt, ct);

  f32x4 acc[4][4], cs[4];
#pragma unroll
  for (int n = 0; n < 4; ++n) {
    cs[n] = f32x4{0.f, 0.f, 0.f, 0.f};
#pragma unroll
    for (int m = 0; m < 4; ++m) acc[m][n] = f32x4{0.f, 0.f, 0.f, 0.f};
  }
  float rsum[2] = {0.f, 0.f}, rsq[2] = {0.f, 0.f};
  const int nq = wave & 3;
  const int ncnt = nq < 3 ? 4 : 3;
  auto colfn = [&](int cw) { return (cw / 10) * 640 + ct * 80 + (cw % 10) * 8; };
  auto pre = [&]() {
    u32x4 pf[5], pf2[5];
    l2_warm<80, 30, 512>(wqkv, 1920, colfn, rt & 7, tid, pf);
    l2_warm<80, 10, 512>(wnext, 640, [&](int cw) { return ct * 80 + cw * 8; }, rt & 7, tid, pf2);
    l2_warm_done(pf);
    l2_warm_done(pf2);
  };
  ln_gemm_loop<240>(smem, x + (size_t)rt * 64 * 640, wqkv, 1920, colfn, pre, wave, lane, nq * 4, ncnt, acc, cs, rsum,
                    rsq);
  ln_gemm_finish(smem, wave, lane, acc, cs, rsum, rsq);

  u16* Qs = reinterpret_cast<u16*>(smem);
  u16* Ks = reinterpret_cast<u16*>(smem + 16384);
  u16* Vt = reinterpret_cast<u16*>(smem + 32768);
  u16* PkT = reinterpret_cast<u16*>(smem + 49152);
  u16* WpT = reinterpret_cast<u16*>(smem + 73728);
  for (int i = tid; i < 2048; i += 512) reinterpret_cast<u32x4*>(smem)[i] = u32x4{0, 0, 0, 0};
  if (tid < 256) Vt[((tid >> 6) * 32 + 20) * 64 + (tid & 63)] = 0x3f80;
  for (int i = tid; i < 4 * 32 * 32; i += 512) {
    int hh = i >> 10, f = (i >> 5) & 31, d = i & 31;
    u16 v = 0;
    if (f < 20 && d < 20) v = wphi[((ct * 4 + hh) * 20 + d) * 20 + f];
    WpT[i] = v;
  }
  __syncthreads();
  if (wave < 4) {
#pragma unroll
    for (int n = 0; n < 4; ++n) {
      int nt = nq * 4 + n;
      if (nt < 15) {
        int part = nt / 5;
        int col = (nt % 5) * 16 + l16;
        int hh = col / 20, d = col % 20;
#pragma unroll
        for (int m = 0; m < 4; ++m) {
          if (part < 2) {
            u16* X = part == 0 ? Qs : Ks;
#pragma unroll
            for (int r = 0; r < 4; ++r) {
              int s = m * 16 + 4 * g + r;
              int ch = (hh * 4 + (d >> 3)) ^ (s & 15);
              X[s * 128 + ch * 8 + (d & 7)] = f2bf(acc[m][n][r]);
            }
          } else {
            u32x2 w = {pack2(acc[m][n][0], acc[m][n][1]), pack2(acc[m][n][2], acc[m][n][3])};
            *reinterpret_cast<u32x2*>(&Vt[(hh * 32 + d) * 64 + m * 16 + 4 * g]) = w;
          }
        }
      }
    }
  }
  __syncthreads();
  for (int t = wave; t < 32; t += 8) {
    int which = t >> 4, hh = (t >> 2) & 3, st = t & 3;
    const u16* X = which == 0 ? Qs : Ks;
    int s = st * 16 + l16;
    bf16x8 b = *reinterpret_cast<const bf16x8*>(&X[s * 128 + ((hh * 4 + g) ^ (s & 15)) * 8]);
    bf16x8 a0 = *reinterpret_cast<const bf16x8*>(&WpT[(hh * 32 + l16) * 32 + g * 8]);
    bf16x8 a1 = *reinterpret_cast<const bf16x8*>(&WpT[(hh * 32 + 16 + l16) * 32 + g * 8]);
    f32x4 c0 = mfma16(a0, b, f32x4{0.f, 0.f, 0.f, 0.f});
    f32x4 c1 = mfma16(a1, b, f32x4{0.f, 0.f, 0.f, 0.f});
    float p0[4], p1[4];
    float mx = -3.0e38f, mn = 3.0e38f;
#pragma unroll
    for (int r = 0; r < 4; ++r) {
      p0[r] = rbf(c0[r]); p1[r] = rbf(c1[r]);
      mx = fmaxf(mx, p0[r]); mn = fminf(mn, p0[r]);
      if (g == 0) { mx = fmaxf(mx, p1[r]); mn = fminf(mn, p1[r]); }
    }
    mx = fmaxf(mx, __shfl_xor(mx, 16)); mx = fmaxf(mx, __shfl_xor(mx, 32));
    mn = fminf(mn, __shfl_xor(mn, 16)); mn = fminf(mn, __shfl_xor(mn, 32));
    float ep0[4], en0[4], ep1[4], en1[4];
    float sp = 0.f, sn = 0.f;
#pragma unroll
    for (int r = 0; r < 4; ++r) {
      ep0[r] = __expf(p0[r] - mx); en0[r] = __expf(mn - p0[r]);
      ep1[r] = g == 0 ? __expf(p1[r] - mx) : 0.f; en1[r] = g == 0 ? __expf(mn - p1[r]) : 0.f;
      sp += ep0[r] + ep1[r]; sn += en0[r] + en1[r];
    }
    sp += __shfl_xor(sp, 16); sp += __shfl_xor(sp, 32);
    sn += __shfl_xor(sn, 16); sn += __shfl_xor(sn, 32);
    const float ip = 1.0f / sp, in_ = 1.0f / sn;
    if (which == 0) {
      u16* dst = phiq + (((size_t)rt * 32 + ct * 4 + hh) * 4 + st) * 640;
      auto fpos = [&](int f) { return f < 32 ? ((f >> 3) * 16 + l16) * 8 + (f & 7) : 512 + l16 * 8 + (f - 32); };
      u32x2 w;
      w = u32x2{pack2(ep0[0] * ip, ep0[1] * ip), pack2(ep0[2] * ip, ep0[3] * ip)};
      *reinterpret_cast<u32x2*>(dst + fpos(4 * g)) = w;
      w = u32x2{pack2(en0[0] * in_, en0[1] * in_), pack2(en0[2] * in_, en0[3] * in_)};
      *reinterpret_cast<u32x2*>(dst + fpos(20 + 4 * g)) = w;
      if (g == 0) {
        w = u32x2{pack2(ep1[0] * ip, ep1[1] * ip), pack2(ep1[2] * ip, ep1[3] * ip)};
        *reinterpret_cast<u32x2*>(dst + fpos(16)) = w;
        w = u32x2{pack2(en1[0] * in_, en1[1] * in_), pack2(en1[2] * in_, en1[3] * in_)};
        *reinterpret_cast<u32x2*>(dst + fpos(36)) = w;
      }
    } else {
#pragma unroll
      for (int r = 0; r < 4; ++r) {
        PkT[(hh * 48 + 4 * g + r) * 64 + s] = f2bf(ep0[r] * ip);
        PkT[(hh * 48 + 20 + 4 * g + r) * 64 + s] = f2bf(en0[r] * in_);
        if (g == 0) {
          PkT[(hh * 48 + 16 + r) * 64 + s] = f2bf(ep1[r] * ip);
          PkT[(hh * 48 + 36 + r) * 64 + s] = f2bf(en1[r] * in_);
        }
      }
    }
  }
  __syncthreads();
  for (int t = wave; t < 24; t += 8) {
    int hh = t / 6, mt = (t % 6) >> 1, nt = t & 1;
    f32x4 c = {0.f, 0.f, 0.f, 0.f};
#pragma unroll
    for (int ks = 0; ks < 2; ++ks) {
      bf16x8 a = *reinterpret_cast<const bf16x8*>(&PkT[(hh * 48 + mt * 16 + l16) * 64 + ks * 32 + g * 8]);
      bf16x8 b = *reinterpret_cast<const bf16x8*>(&Vt[(hh * 32 + nt * 16 + l16) * 64 + ks * 32 + g * 8]);
      c = mfma16(a, b, c);
    }
    int d = nt * 16 + l16;
    if (d < 21) {
#pragma unroll
      for (int r = 0; r < 4; ++r) {
        int f = mt * 16 + 4 * g + r;
        if (f < 40) kvpart[(((size_t)rt * 32 + ct * 4 + hh) * 21 + d) * 40 + f] = c[r];
      }
    }
  }
}

__global__ __launch_bounds__(256) void kr_reduce(const float* __restrict__ kvpart, u16* __restrict__ kvzT) {
  int idx = blockIdx.x * 256 + threadIdx.x;
  int h = idx >> 11, d = ((idx >> 10) & 1) * 16 + ((idx >> 3) & 15), f = ((idx >> 9) & 1) * 32 + ((idx >> 7) & 3) * 8 + (idx & 7);
  float s = 0.f;
  if (d < 21 && f < 40) {
    const float* p = kvpart + ((size_t)h * 21 + d) * 40 + f;
    float v[32];
#pragma unroll
    for (int rt = 0; rt < 32; ++rt) v[rt] = p[(size_t)rt * 32 * 840];
#pragma unroll
    for (int rt = 0; rt < 32; ++rt) s += v[rt];
  }
  kvzT[idx] = f2bf(s);
}

constexpr int K2_B_OFF = 81920;
constexpr int K2_BSTG = 128 * 160;
constexpr int K2_NBUF = 3;
constexpr int K2_LDS = K2_B_OFF + K2_NBUF * K2_BSTG;

__global__ __launch_bounds__(1024) void k2_attn_out(const u16* __restrict__ phiq, const u16* __restrict__ kvzT,
                                                    const u16* __restrict__ wo, const u16* __restrict__ x,
                                                    u16* __restrict__ hbuf, const u16* __restrict__ wnext) {
  extern __shared__ __attribute__((aligned(16))) char smem[];
  const int tid = threadIdx.x, wave = tid >> 6, lane = tid & 63;
  const int g = lane >> 4, l16 = lane & 15;
  int rt, ct;
  tile_map(blockIdx.x, rt, ct);
  char* As = smem;
  char* Bs = smem + K2_B_OFF;

  const u16* bsrc[2];
#pragma unroll
  for (int i = 0; i < 2; ++i) {
    int p = (wave + 16 * i) * 64 + lane;
    int kp = p / 10, cw = p % 10;
    bsrc[i] = wo + (size_t)swap23(kp) * 640 + ct * 80 + cw * 8;
  }
  auto issue = [&](int c) {
    char* buf = Bs + (c % K2_NBUF) * K2_BSTG;
    glds16(bsrc[0] + (size_t)c * 128 * 640, buf + wave * 1024);
    if (wave < 4) glds16(bsrc[1] + (size_t)c * 128 * 640, buf + (wave + 16) * 1024);
  };
  issue(0); issue(1);

  {
    bf16x8 ka[2][2][2], b0[2][4], b1[2][4];
#pragma unroll
    for (int hi = 0; hi < 2; ++hi) {
      const int h = wave + 16 * hi;
#pragma unroll
      for (int st = 0; st < 4; ++st) {
        const u16* pq = phiq + (((size_t)rt * 32 + h) * 4 + st) * 640;
        b0[hi][st] = *reinterpret_cast<const bf16x8*>(pq + lane * 8);
        b1[hi][st] = *reinterpret_cast<const bf16x8*>(pq + 512 + l16 * 8);
      }
#pragma unroll
      for (int mt = 0; mt < 2; ++mt)
#pragma unroll
        for (int ks = 0; ks < 2; ++ks)
          ka[hi][mt][ks] =
              *reinterpret_cast<const bf16x8*>(kvzT + ((((size_t)h * 2 + mt) * 2 + ks) * 64 + lane) * 8);
    }
#pragma unroll
    for (int hi = 0; hi < 2; ++hi) {
      const int h = wave + 16 * hi;
#pragma unroll
      for (int st = 0; st < 4; ++st) {
        int s = st * 16 + l16;
        bf16x8 bb1 = b1[hi][st];
        if (g != 0) bb1 = bf16x8{0, 0, 0, 0, 0, 0, 0, 0};
        f32x4 c0 = {0.f, 0.f, 0.f, 0.f}, c1 = {0.f, 0.f, 0.f, 0.f};
        c0 = mfma16(ka[hi][0][0], b0[hi][st], c0); c0 = mfma16(ka[hi][0][1], bb1, c0);
        c1 = mfma16(ka[hi][1][0], b0[hi][st], c1); c1 = mfma16(ka[hi][1][1], bb1, c1);
        float den = rbf(rbf(__shfl(c1[0], 16 + l16)) + 1e-6f);
        float inv = 1.0f / den;
        u32x2 w = {pack2(rbf(c0[0]) * inv, rbf(c0[1]) * inv), pack2(rbf(c0[2]) * inv, rbf(c0[3]) * inv)};
        *reinterpret_cast<u32x2*>(As + a640_off(s, h * 20 + 4 * g)) = w;
        if (g == 0) {
          u32x2 w2 = {pack2(rbf(c1[0]) * inv, rbf(c1[1]) * inv), pack2(rbf(c1[2]) * inv, rbf(c1[3]) * inv)};
          *reinterpret_cast<u32x2*>(As + a640_off(s, h * 20 + 16)) = w2;
        }
      }
    }
  }

  u32x4 pf[5];
  {
    const int n0 = (ct >> 1) * 432 + (ct & 1) * 224;
    l2_warm<80, 28, 1024>(wnext, 1728, [&](int cw) { int col = n0 + cw * 8; return col > 1720 ? 1720 : col; }, rt & 7, tid, pf);
  }
  f32x4 acc[2][5];
#pragma unroll
  for (int m = 0; m < 2; ++m)
#pragma unroll
    for (int n = 0; n < 5; ++n) acc[m][n] = f32x4{0.f, 0.f, 0.f, 0.f};
  const int kq = wave & 3, mh = (wave >> 2) & 1;
  const int q4 = (lane & 15) >> 2, p4 = lane & 3;
  const int krow = 16 * (g >> 1) + 4 * (g & 1) + q4;
  const unsigned boff = lds_addr(Bs) + (kq * 32 + krow) * 160 + 4 * p4 * 2;
  asm volatile("s_waitcnt lgkmcnt(0)" ::: "memory");

  for (int c = 0; c < 5; ++c) {
    if (c + K2_NBUF - 2 < 5) {
      if (wave < 4) wait_vm<2 * (K2_NBUF - 2)>(); else wait_vm<(K2_NBUF - 2)>();
    } else wait_vm<0>();
    raw_barrier();
    if (c + K2_NBUF - 1 < 5) issue(c + K2_NBUF - 1);
    if (wave < 8) {
      const unsigned ba = boff + (c % K2_NBUF) * K2_BSTG;
      const unsigned aa = lds_addr(As + a640_off(mh * 32 + l16, c * 128 + kq * 32 + g * 8));
      bf16x8 a0 = b128_rd<0>(aa), a1 = b128_rd<20480>(aa);
      bf16x4 t[5][2];
      t[0][0] = tr_rd<0>(ba); t[0][1] = tr_rd<1280>(ba);
      t[1][0] = tr_rd<32>(ba); t[1][1] = tr_rd<1312>(ba);
      t[2][0] = tr_rd<64>(ba); t[2][1] = tr_rd<1344>(ba);
      t[3][0] = tr_rd<96>(ba); t[3][1] = tr_rd<1376>(ba);
      t[4][0] = tr_rd<128>(ba); t[4][1] = tr_rd<1408>(ba);
      lds_wait();
#pragma unroll
      for (int n = 0; n < 5; ++n) {
        bf16x8 b = cat(t[n][0], t[n][1]);
        acc[0][n] = mfma16(a0, b, acc[0][n]);
        acc[1][n] = mfma16(a1, b, acc[1][n]);
      }
    }
  }
  __syncthreads();
  f32x4* red = reinterpret_cast<f32x4*>(smem);
  if (wave < 8) {
#pragma unroll
    for (int m = 0; m < 2; ++m)
#pragma unroll
      for (int n = 0; n < 5; ++n) red[((kq * 4 + mh * 2 + m) * 5 + n) * 64 + lane] = acc[m][n];
  }
  __syncthreads();
  for (int t = wave; t < 20; t += 16) {
    int m = t / 5, n = t % 5;
    f32x4 v = red[((0 * 4 + m) * 5 + n) * 64 + lane];
#pragma unroll
    for (int w = 1; w < 4; ++w) v += red[((w * 4 + m) * 5 + n) * 64 + lane];
    float xv[4];
#pragma unroll
    for (int r = 0; r < 4; ++r) xv[r] = bf2f(x[(size_t)(rt * 64 + m * 16 + 4 * g + r) * 640 + ct * 80 + n * 16 + l16]);
#pragma unroll
    for (int r = 0; r < 4; ++r)
      hbuf[(size_t)(rt * 64 + m * 16 + 4 * g + r) * 640 + ct * 80 + n * 16 + l16] = f2bf(xv[r] + rbf(v[r]));
  }
  l2_warm_done(pf);
}

constexpr int K3_LDS = 4 * (8192 + 64 * 448);

__global__ __launch_bounds__(512) void k3_ffn1(const u16* __restrict__ hbuf, const u16* __restrict__ w1,
                                                u16* __restrict__ act, const u16* __restrict__ wnext) {
  extern __shared__ __attribute__((aligned(16))) char smem[];
  const int tid = threadIdx.x, wave = tid >> 6, lane = tid & 63;
  const int g = lane >> 4, l16 = lane & 15;
  int rt, ct;
  tile_map(blockIdx.x, rt, ct);
  const int n0 = (ct >> 1) * 432 + (ct & 1) * 224;
  const int ntiles = (ct & 1) ? 13 : 14;

  f32x4 acc[4][4], cs[4];
#pragma unroll
  for (int n = 0; n < 4; ++n) {
    cs[n] = f32x4{0.f, 0.f, 0.f, 0.f};
#pragma unroll
    for (int m = 0; m < 4; ++m) acc[m][n] = f32x4{0.f, 0.f, 0.f, 0.f};
  }
  float rsum[2] = {0.f, 0.f}, rsq[2] = {0.f, 0.f};
  const int nq = wave & 3;
  const int nbase = nq < 2 ? nq * 4 : 8 + (nq - 2) * 3;
  const int ncnt = nq < 2 ? 4 : 3;
  auto colfn = [&](int cw) { int col = n0 + cw * 8; return col > 1720 ? 1720 : col; };
  auto pre = [&]() {
    u32x4 pf[5];
    l2_warm<216, 10, 512>(wnext, 640, [&](int cw) { return ct * 80 + cw * 8; }, rt & 7, tid, pf);
    l2_warm_done(pf);
  };
  ln_gemm_loop<224>(smem, hbuf + (size_t)rt * 64 * 640, w1, 1728, colfn, pre, wave, lane, nbase, ncnt, acc, cs, rsum,
                    rsq);
  ln_gemm_finish(smem, wave, lane, acc, cs, rsum, rsq);
  if (wave < 4) {
#pragma unroll
    for (int n = 0; n < 4; ++n) {
      int nt = nbase + n;
      if (n < ncnt && nt < ntiles) {
#pragma unroll
        for (int m = 0; m < 4; ++m)
#pragma unroll
          for (int r = 0; r < 4; ++r) {
            float v = rbf(acc[m][n][r]);
            float sv = v / (1.0f + __expf(-v));
            act[(size_t)(rt * 64 + m * 16 + 4 * g + r) * 1728 + n0 + nt * 16 + l16] = f2bf(sv);
          }
      }
    }
  }
}

constexpr int K4_STG = 16384 + 20480;
constexpr int K4_NBUF = 4;
constexpr int K4_LDS = K4_NBUF * K4_STG;

__global__ __launch_bounds__(1024) void k4_ffn2(const u16* __restrict__ act, const u16* __restrict__ w2,
                                                const u16* __restrict__ hbuf, u16* __restrict__ out) {
  extern __shared__ __attribute__((aligned(16))) char smem[];
  const int tid = threadIdx.x, wave = tid >> 6, lane = tid & 63;
  const int g = lane >> 4, l16 = lane & 15;
  int rt, ct;
  tile_map(blockIdx.x, rt, ct);

  const u16* asrc;
  const u16* bsrc[2];
  int bk[2];
  {
    int p = wave * 64 + lane;
    int row = p >> 4, c16 = p & 15;
    asrc = act + (size_t)(rt * 64 + row) * 1728 + ((c16 ^ (row & 15)) * 8);
  }
#pragma unroll
  for (int i = 0; i < 2; ++i) {
    int p = (i == 0 ? wave : 8 + wave) * 64 + lane;
    int kp = p / 10, cw = p % 10;
    bk[i] = swap23(kp);
    bsrc[i] = w2 + ct * 80 + cw * 8;
  }
  const bool extra = wave >= 8 && wave < 12;
  auto issue = [&](int c) {
    char* buf = smem + (c & (K4_NBUF - 1)) * K4_STG;
    glds16(asrc + c * 128, buf + wave * 1024);
    int k = c * 128 + bk[0];
    k = k > 1727 ? 1727 : k;
    glds16(bsrc[0] + (size_t)k * 640, buf + 16384 + wave * 1024);
    if (extra) {
      k = c * 128 + bk[1];
      k = k > 1727 ? 1727 : k;
      glds16(bsrc[1] + (size_t)k * 640, buf + 16384 + (8 + wave) * 1024);
    }
  };
  issue(0); issue(1); issue(2);

  f32x4 acc[2][5];
#pragma unroll
  for (int m = 0; m < 2; ++m)
#pragma unroll
    for (int n = 0; n < 5; ++n) acc[m][n] = f32x4{0.f, 0.f, 0.f, 0.f};
  const int kq = wave & 3, mh = (wave >> 2) & 1;
  const int q4 = (lane & 15) >> 2, p4 = lane & 3;
  const int krow = 16 * (g >> 1) + 4 * (g & 1) + q4;
  const unsigned aoff = lds_addr(smem) + (mh * 32 + l16) * 256 + (((kq * 4 + g) ^ l16) * 16);
  const unsigned boff = lds_addr(smem) + 16384 + (kq * 32 + krow) * 160 + 4 * p4 * 2;

  for (int c = 0; c < 14; ++c) {
    if (c + K4_NBUF - 2 < 14) {
      if (extra) wait_vm<3 * (K4_NBUF - 2)>(); else wait_vm<2 * (K4_NBUF - 2)>();
    } else wait_vm<0>();
    raw_barrier();
    if (c + K4_NBUF - 1 < 14) issue(c + K4_NBUF - 1);
    if (wave < 8 && (c < 13 || kq < 2)) {
      const unsigned so = (c & (K4_NBUF - 1)) * K4_STG;
      const unsigned aa = aoff + so, ba = boff + so;
      bf16x8 a0 = b128_rd<0>(aa), a1 = b128_rd<4096>(aa);
      bf16x4 t[5][2];
      t[0][0] = tr_rd<0>(ba); t[0][1] = tr_rd<1280>(ba);
      t[1][0] = tr_rd<32>(ba); t[1][1] = tr_rd<1312>(ba);
      t[2][0] = tr_rd<64>(ba); t[2][1] = tr_rd<1344>(ba);
      t[3][0] = tr_rd<96>(ba); t[3][1] = tr_rd<1376>(ba);
      t[4][0] = tr_rd<128>(ba); t[4][1] = tr_rd<1408>(ba);
      lds_wait();
#pragma unroll
      for (int n = 0; n < 5; ++n) {
        bf16x8 b = cat(t[n][0], t[n][1]);
        acc[0][n] = mfma16(a0, b, acc[0][n]);
        acc[1][n] = mfma16(a1, b, acc[1][n]);
      }
    }
  }
  __syncthreads();
  f32x4* red = reinterpret_cast<f32x4*>(smem);
  if (wave < 8) {
#pragma unroll
    for (int m = 0; m < 2; ++m)
#pragma unroll
      for (int n = 0; n < 5; ++n) red[((kq * 4 + mh * 2 + m) * 5 + n) * 64 + lane] = acc[m][n];
  }
  __syncthreads();
  for (int t = wave; t < 20; t += 16) {
    int m = t / 5, n = t % 5;
    f32x4 v = red[((0 * 4 + m) * 5 + n) * 64 + lane];
#pragma unroll
    for (int w = 1; w < 4; ++w) v += red[((w * 4 + m) * 5 + n) * 64 + lane];
    float hv[4];
#pragma unroll
    for (int r = 0; r < 4; ++r) hv[r] = bf2f(hbuf[(size_t)(rt * 64 + m * 16 + 4 * g + r) * 640 + ct * 80 + n * 16 + l16]);
#pragma unroll
    for (int r = 0; r < 4; ++r)
      out[(size_t)(rt * 64 + m * 16 + 4 * g + r) * 640 + ct * 80 + n * 16 + l16] = f2bf(hv[r] + rbf(v[r]));
  }
}

extern "C" void kernel_launch(void* const* d_in, const int* in_sizes, int n_in, void* d_out, int out_size,
                              void* d_ws, size_t ws_size, hipStream_t stream) {
  const u16* x = (const u16*)d_in[0];
  const u16* wqkv = (const u16*)d_in[1];
  const u16* wphi = (const u16*)d_in[2];
  const u16* wo = (const u16*)d_in[3];
  const u16* w1 = (const u16*)d_in[5];
  const u16* w2 = (const u16*)d_in[6];
  u16* out = (u16*)d_out;

  char* ws = (char*)d_ws;
  u16* phiq = (u16*)(ws);
  float* kvpart = (float*)(ws + 5242880);
  u16* kvzT = (u16*)(ws + 5242880 + 3440640);
  u16* hbuf = (u16*)(ws + 8814592);
  u16* act = (u16*)(ws + 8814592 + 2621440);

  hipFuncSetAttribute((const void*)k1_qkv, hipFuncAttributeMaxDynamicSharedMemorySize, K1_LDS);
  hipFuncSetAttribute((const void*)k2_attn_out, hipFuncAttributeMaxDynamicSharedMemorySize, K2_LDS);
  hipFuncSetAttribute((const void*)k3_ffn1, hipFuncAttributeMaxDynamicSharedMemorySize, K3_LDS);
  hipFuncSetAttribute((const void*)k4_ffn2, hipFuncAttributeMaxDynamicSharedMemorySize, K4_LDS);

  k1_qkv<<<256, 512, K1_LDS, stream>>>(x, wqkv, wphi, phiq, kvpart, wo);
  kr_reduce<<<256, 256, 0, stream>>>(kvpart, kvzT);
  k2_attn_out<<<256, 1024, K2_LDS, stream>>>(phiq, kvzT, wo, x, hbuf, w1);
  k3_ffn1<<<256, 512, K3_LDS, stream>>>(hbuf, w1, act, w2);
  k4_ffn2<<<256, 1024, K4_LDS, stream>>>(act, w2, hbuf, out);
}
```
